```python
import torch

def _softmax(x, axis=-1):
    return torch.softmax(x, dim=axis)
def _gelu(x):
    return torch.nn.functional.gelu(x, approximate="tanh")
def _silu(x):
    return torch.nn.functional.silu(x)
def _sigmoid(x):
    return torch.sigmoid(x)
def _layernorm(x, axis=-1, eps=1e-5):
    m = x.mean(dim=axis, keepdim=True)
    v = x.var(dim=axis, keepdim=True, unbiased=False)
    return (x - m) / torch.sqrt(v + eps)
def _scatter_add(tgt, idx, src, axis=0):
    return tgt.clone().index_add_(axis, idx, src)
def _segment_sum(src, seg, n):
    out = torch.zeros((n,) + src.shape[1:], dtype=src.dtype)
    return out.index_add_(0, seg, src)
def _pad(x, pw):
    flat = tuple(v for pair in reversed(pw) for v in pair)
    return torch.nn.functional.pad(x, flat)

kernel_name = 'concat_heads__mha_block_cached__gqa_block_cached__near_ridge'

def setup_inputs():

    _w = (lambda a: (lambda t: t.to(torch.bfloat16) if t.is_floating_point() else t)(torch.as_tensor(a)))
    def _A_setup():
        B, Sq, D = 16, 144, 576
        H, Sk, Dh = 32, 512, 128
        return {
            "x": _w((lambda *s: torch.randn(*s, dtype=torch.bfloat16) * 0.8)(B, Sq, D)),
            "k_cache": _w((lambda *s: torch.randn(*s, dtype=torch.bfloat16) * 0.8)(B, H, Sk, Dh)),
            "v_cache": _w((lambda *s: torch.randn(*s, dtype=torch.bfloat16) * 0.05)(B, H, Sk, Dh)),
            "wq": _w((lambda *s: torch.randn(*s, dtype=torch.bfloat16) * 0.8)(D, H * Dh) * (D ** -0.5)),
            "wo": _w((lambda *s: torch.randn(*s, dtype=torch.bfloat16) * 0.05)(H * Dh, D) * ((H * Dh) ** -0.5)),
        }
    _A_in = _A_setup()

    def _B_setup():
        B, Sq, D = 16, 144, 576
        Hq, Hkv, Sk, Dh = 32, 32, 512, 128
        assert Hq % Hkv == 0, (Hq, Hkv)
        return {
            "x": _w((lambda *s: torch.randn(*s, dtype=torch.bfloat16) * 0.8)(B, Sq, D)),
            "k_cache": _w((lambda *s: torch.randn(*s, dtype=torch.bfloat16) * 0.8)(B, Hkv, Sk, Dh)),
            "v_cache": _w((lambda *s: torch.randn(*s, dtype=torch.bfloat16) * 0.05)(B, Hkv, Sk, Dh)),
            "wq": _w((lambda *s: torch.randn(*s, dtype=torch.bfloat16) * 0.8)(D, Hq * Dh) * (D ** -0.5)),
            "wo": _w((lambda *s: torch.randn(*s, dtype=torch.bfloat16) * 0.05)(Hq * Dh, D) * ((Hq * Dh) ** -0.5)),
        }
    _B_in = _B_setup()

    return {
        "A_x": _A_in["x"],
        "A_k_cache": _A_in["k_cache"],
        "A_v_cache": _A_in["v_cache"],
        "A_wq": _A_in["wq"],
        "A_wo": _A_in["wo"],
        "B_x": _B_in["x"],
        "B_k_cache": _B_in["k_cache"],
        "B_v_cache": _B_in["v_cache"],
        "B_wq": _B_in["wq"],
        "B_wo": _B_in["wo"],
    }

def reference(A_x, A_k_cache, A_v_cache, A_wq, A_wo, B_x, B_k_cache, B_v_cache, B_wq, B_wo):
    def _A_ref(x, k_cache, v_cache, wq, wo):
        B, Sq, D = x.shape
        H, Dh = 32, 128
        q = (lambda x, *d: x.permute(*d))((x @ wq).reshape(B, Sq, H, Dh), 0, 2, 1, 3)
        scores = torch.einsum("bhqd,bhkd->bhqk", q, k_cache) * (Dh ** -0.5)
        p = _softmax(scores, axis=-1)
        o = torch.einsum("bhqk,bhkd->bhqd", p, v_cache)
        return (lambda x, *d: x.permute(*d))(o, 0, 2, 1, 3).reshape(B, Sq, H * Dh) @ wo

    def _B_ref(x, k_cache, v_cache, wq, wo):
        B, Sq, D = x.shape
        Hq, Dh = 32, 128
        Hkv = k_cache.shape[1]
        rep = Hq // Hkv
        q = (lambda x, *d: x.permute(*d))((x @ wq).reshape(B, Sq, Hq, Dh), 0, 2, 1, 3)
        k = (lambda x, n, axis: torch.repeat_interleave(x, n, dim=axis))(k_cache, rep, axis=1)
        v = (lambda x, n, axis: torch.repeat_interleave(x, n, dim=axis))(v_cache, rep, axis=1)
        scores = torch.einsum("bhqd,bhkd->bhqk", q, k) * (Dh ** -0.5)
        p = _softmax(scores, axis=-1)
        o = torch.einsum("bhqk,bhkd->bhqd", p, v)
        return (lambda x, *d: x.permute(*d))(o, 0, 2, 1, 3).reshape(B, Sq, Hq * Dh) @ wo

    a = _A_ref(A_x, A_k_cache, A_v_cache, A_wq, A_wo)
    b = _B_ref(B_x, B_k_cache, B_v_cache, B_wq, B_wo)
    return torch.concatenate([a, b], axis=-1)
```

```cpp
#include <hip/hip_runtime.h>
#include <stdint.h>

typedef __bf16 bf16x8 __attribute__((ext_vector_type(8)));
typedef __bf16 bf16x4 __attribute__((ext_vector_type(4)));
typedef __bf16 bf16x2 __attribute__((ext_vector_type(2)));
typedef float f32x4 __attribute__((ext_vector_type(4)));
typedef float f32x2 __attribute__((ext_vector_type(2)));
typedef unsigned u32x4 __attribute__((ext_vector_type(4)));
typedef unsigned u32x2 __attribute__((ext_vector_type(2)));
typedef __amdgpu_buffer_rsrc_t srd_t;
#define LAS __attribute__((address_space(3)))
#define GAS __attribute__((address_space(1)))
#define DI __device__ __forceinline__

constexpr int SQ = 144, DM = 576, NH = 32, SK = 512, DH = 128, NB = 16;
constexpr float QSCALE = 0.088388347648318440f * 1.4426950408889634f;
constexpr float THR = 8.0f;

constexpr size_t WS_O = 0;
constexpr size_t WS_O_BYTES = (size_t)2 * 144 * 128 * 1024;
constexpr size_t WS_WO = WS_O + WS_O_BYTES;
constexpr size_t WS_WO_BYTES = (size_t)2 * 36 * 128 * 1024;
constexpr size_t WS_PART = WS_WO + WS_WO_BYTES;
constexpr size_t WS_PART_BYTES = (size_t)256 * 81 * 1024;
constexpr size_t WS_FLAG = WS_PART + WS_PART_BYTES;
constexpr size_t WS_FLAG_BYTES = (size_t)256 * 4 * 128;
constexpr size_t WS_END = WS_FLAG + WS_FLAG_BYTES;

DI unsigned rfl(unsigned x) { return __builtin_amdgcn_readfirstlane(x); }
DI srd_t make_srd(const void* p, unsigned bytes) { return __builtin_amdgcn_make_buffer_rsrc((void*)p, (short)0, (int)bytes, 0x00020000); }
DI unsigned cvtpk(float lo, float hi) { f32x2 v = {lo, hi}; bf16x2 b = __builtin_convertvector(v, bf16x2); return __builtin_bit_cast(unsigned, b); }
DI f32x4 mfma(bf16x8 a, bf16x8 b, f32x4 c) { return __builtin_amdgcn_mfma_f32_16x16x32_bf16(a, b, c, 0, 0, 0); }
DI void dma(unsigned dst, unsigned voff, srd_t srd, unsigned soff) {
  asm volatile("s_mov_b32 m0, %0\n\ts_nop 0\n\tbuffer_load_dwordx4 %1, %2, %3 offen lds" ::"s"(dst), "v"(voff), "s"(srd), "s"(soff) : "memory");
}
template <int N> DI void wait_bar() { asm volatile("s_waitcnt vmcnt(%0) lgkmcnt(0)\n\ts_barrier" ::"i"(N) : "memory"); }
DI bf16x8 lds_b128(unsigned addr) { return *(LAS bf16x8*)addr; }
DI bf16x8 lds_tr2(unsigned a0, unsigned a1) {
  bf16x4 lo = __builtin_amdgcn_ds_read_tr16_b64_v4bf16((LAS bf16x4*)a0);
  bf16x4 hi = __builtin_amdgcn_ds_read_tr16_b64_v4bf16((LAS bf16x4*)a1);
  return __builtin_shufflevector(lo, hi, 0, 1, 2, 3, 4, 5, 6, 7);
}
DI int imax3(int a, int b, int c) { return max(max(a, b), c); }

struct K1Params {
  const void* x[2]; const void* k[2]; const void* v[2]; const void* wq[2]; const void* wo[2];
  char* ws;
};

constexpr unsigned UNIT = 16384, NSLOT = 5, K1_LDS = UNIT * NSLOT;

template <int NT> struct K1State {
  f32x4 O[8][NT], L[NT], NM[NT];
  bf16x8 Q[4][NT];
};

struct K1Ctx {
  srd_t sK, sV, sWn;
  unsigned vK0, vK1, vV, vW;
  unsigned kb[4], vb0, vb1;
  unsigned dstw;
  unsigned hoffn;
  unsigned slot;
};
DI unsigned next_slot(unsigned s) { s += UNIT; return s == K1_LDS ? 0u : s; }
DI unsigned prev_slot(unsigned s) { return (s == 0u ? K1_LDS : s) - UNIT; }

DI void issue_w(const K1Ctx& c, srd_t sW, unsigned hoff, int unit, unsigned slot) {
  const unsigned so = (unsigned)unit * 64u * 8192u + hoff, d = slot + c.dstw;
  dma(d, c.vW, sW, so);
  dma(d + 1024, c.vW, sW, so + 128);
  dma(d + 8192, c.vW, sW, so + 32 * 8192);
  dma(d + 8192 + 1024, c.vW, sW, so + 32 * 8192 + 128);
}
DI void issue_kv(const K1Ctx& c, int j, unsigned slot) {
  const unsigned sk = (unsigned)min(j, 15) * 8192u, sv = (unsigned)max(j - 1, 0) * 8192u, d = slot + c.dstw;
  dma(d, c.vK0, c.sK, sk);
  dma(d + 1024, c.vK1, c.sK, sk);
  dma(d + 8192, c.vV, c.sV, sv);
  dma(d + 8192 + 1024, c.vV, c.sV, sv + 128);
}

template <int NT> DI void qk(f32x4 (&S)[2][NT], const K1State<NT>& st, const K1Ctx& c, bool first) {
#pragma unroll
  for (int sg = 0; sg < 2; ++sg)
#pragma unroll
    for (int s = 0; s < 4; ++s) {
      const bf16x8 a = lds_b128(c.kb[s] + c.slot + sg * 4096);
#pragma unroll
      for (int t = 0; t < NT; ++t) {
        const f32x4 ci = s == 0 ? (first ? f32x4{0.f, 0.f, 0.f, 0.f} : st.NM[t]) : S[sg][t];
        S[sg][t] = mfma(a, st.Q[s][t], ci);
      }
    }
}
template <int NT> DI void pv(const f32x4 (&S)[2][NT], K1State<NT>& st, const K1Ctx& c) {
  bf16x8 P[NT];
#pragma unroll
  for (int t = 0; t < NT; ++t) {
    u32x4 w;
    w[0] = cvtpk(__builtin_amdgcn_exp2f(S[0][t][0]), __builtin_amdgcn_exp2f(S[0][t][1]));
    w[1] = cvtpk(__builtin_amdgcn_exp2f(S[0][t][2]), __builtin_amdgcn_exp2f(S[0][t][3]));
    w[2] = cvtpk(__builtin_amdgcn_exp2f(S[1][t][0]), __builtin_amdgcn_exp2f(S[1][t][1]));
    w[3] = cvtpk(__builtin_amdgcn_exp2f(S[1][t][2]), __builtin_amdgcn_exp2f(S[1][t][3]));
    P[t] = __builtin_bit_cast(bf16x8, w);
  }
  const u32x4 onesw = {0x3F803F80u, 0x3F803F80u, 0x3F803F80u, 0x3F803F80u};
  const bf16x8 ones = __builtin_bit_cast(bf16x8, onesw);
#pragma unroll
  for (int t = 0; t < NT; ++t) st.L[t] = mfma(ones, P[t], st.L[t]);
#pragma unroll
  for (int cc = 0; cc < 8; ++cc) {
    const unsigned b = ((cc & 1) ? c.vb1 : c.vb0) + c.slot + 8192 + (cc >> 1) * 512;
    const bf16x8 a = lds_tr2(b, b + 4096);
#pragma unroll
    for (int t = 0; t < NT; ++t) st.O[cc][t] = mfma(a, P[t], st.O[cc][t]);
  }
}
DI float xmax(float r) {
  r = fmaxf(r, __shfl_xor(r, 16));
  r = fmaxf(r, __shfl_xor(r, 32));
  return r;
}
template <int NT> DI float tile_max(const f32x4 (&S)[2][NT], int t) {
  float r = fmaxf(fmaxf(S[0][t][0], S[0][t][1]), fmaxf(S[0][t][2], S[0][t][3]));
  r = fmaxf(r, fmaxf(fmaxf(S[1][t][0], S[1][t][1]), fmaxf(S[1][t][2], S[1][t][3])));
  return xmax(r);
}
template <int NT> DI void init_max(f32x4 (&S)[2][NT], K1State<NT>& st) {
#pragma unroll
  for (int t = 0; t < NT; ++t) {
    const float r = tile_max<NT>(S, t);
    S[0][t] -= r; S[1][t] -= r;
    st.NM[t] = f32x4{-r, -r, -r, -r};
    st.L[t] = f32x4{0.f, 0.f, 0.f, 0.f};
#pragma unroll
    for (int cc = 0; cc < 8; ++cc) st.O[cc][t] = f32x4{0.f, 0.f, 0.f, 0.f};
  }
}
template <int NT> DI void check_max(f32x4 (&S)[2][NT], K1State<NT>& st) {
  int mx = 0;
#pragma unroll
  for (int t = 0; t < NT; ++t)
#pragma unroll
    for (int sg = 0; sg < 2; ++sg) {
      mx = imax3(mx, __float_as_int(S[sg][t][0]), __float_as_int(S[sg][t][1]));
      mx = imax3(mx, __float_as_int(S[sg][t][2]), __float_as_int(S[sg][t][3]));
    }
  if (__builtin_expect(__builtin_amdgcn_ballot_w64(mx > __float_as_int(THR)) != 0ull, 0)) {
#pragma unroll
    for (int t = 0; t < NT; ++t) {
      const float dl = fmaxf(tile_max<NT>(S, t), 0.f);
      const float f = __builtin_amdgcn_exp2f(-dl);
      S[0][t] -= dl; S[1][t] -= dl;
      st.NM[t] -= dl;
      st.L[t] *= f;
#pragma unroll
      for (int cc = 0; cc < 8; ++cc) st.O[cc][t] *= f;
    }
  }
}

template <int NT, int S> DI void load_x(bf16x8 (&X)[2][NT], unsigned vX, srd_t sX, const unsigned (&sot)[NT]) {
#pragma unroll
  for (int u = 0; u < 2; ++u)
#pragma unroll
    for (int t = 0; t < NT; ++t) {
      if (u == 0) asm volatile("buffer_load_dwordx4 %0, %1, %2, %3 offen offset:%4" : "=v"(X[u][t]) : "v"(vX), "s"(sX), "s"(sot[t]), "i"(S * 128));
      else        asm volatile("buffer_load_dwordx4 %0, %1, %2, %3 offen offset:%4" : "=v"(X[u][t]) : "v"(vX), "s"(sX), "s"(sot[t]), "i"(S * 128 + 64));
    }
}
template <int NT> DI void pin_x(bf16x8 (&X)[2][NT]) {
  if constexpr (NT == 3) asm volatile("" : "+v"(X[0][0]), "+v"(X[0][1]), "+v"(X[0][2]), "+v"(X[1][0]), "+v"(X[1][1]), "+v"(X[1][2]));
  else                   asm volatile("" : "+v"(X[0][0]), "+v"(X[0][1]), "+v"(X[1][0]), "+v"(X[1][1]));
}
template <int NT, int S> constexpr int qwait() {
  return S == 0 ? 6 * NT : S == 1 ? 6 * NT + 4 : S == 2 ? 6 * NT + 8 : S <= 5 ? 6 * NT + 12 : S == 6 ? 4 * NT + 12 : S == 7 ? 2 * NT + 12 : 12;
}
template <int NT, int S>
DI void qstep(f32x4 (&A)[8][NT], bf16x8 (&X)[2][NT], K1Ctx& c, srd_t sW, unsigned hoff, unsigned vX, srd_t sX, const unsigned (&sot)[NT]) {
  wait_bar<qwait<NT, S>()>();
  pin_x<NT>(X);
  if constexpr (S + 4 <= 8) issue_w(c, sW, hoff, S + 4, prev_slot(c.slot));
  else issue_kv(c, S + 4 - 9, prev_slot(c.slot));
#pragma unroll
  for (int u = 0; u < 2; ++u)
#pragma unroll
    for (int cc = 0; cc < 8; ++cc) {
      const unsigned b = ((cc & 1) ? c.vb1 : c.vb0) + c.slot + u * 8192 + (cc >> 1) * 512;
      const bf16x8 a = lds_tr2(b, b + 4096);
#pragma unroll
      for (int t = 0; t < NT; ++t) {
        const f32x4 ci = (S == 0 && u == 0) ? f32x4{0.f, 0.f, 0.f, 0.f} : A[cc][t];
        A[cc][t] = mfma(a, X[u][t], ci);
      }
    }
  if constexpr (S + 4 <= 8) load_x<NT, S + 4>(X, vX, sX, sot);
  c.slot = next_slot(c.slot);
}

template <int NT> DI void k1_body(const K1Params& prm, const int t0, const int wave, const int lane) {
  const int g = lane >> 4, r = lane & 15;
  const int xcd = blockIdx.x & 7, idx = blockIdx.x >> 3;
  const int b = (xcd & 1) * 8 + (idx >> 3), h = (xcd >> 1) * 8 + (idx & 7);
  K1Ctx c;
  {
    const int rw = lane >> 4;
    c.vK0 = (8 * wave + rw) * 256 + (((lane & 15) ^ (8 * (wave & 1) + rw)) << 4);
    c.vK1 = (8 * wave + 4 + rw) * 256 + (((lane & 15) ^ (8 * (wave & 1) + 4 + rw)) << 4);
    const int sub = lane >> 5, vr = (lane >> 2) & 7, ch = (lane & 3) ^ ((vr >> 2) << 1);
    c.vV = (8 * wave + vr) * 256 + sub * 64 + ch * 16;
    const int kk = 16 * (wave & 1) + 8 * (vr >> 2) + 4 * (wave >> 1) + (vr & 3);
    c.vW = kk * 8192 + sub * 64 + ch * 16;
#pragma unroll
    for (int s = 0; s < 4; ++s) c.kb[s] = r * 256 + (((4 * s + g) ^ r) << 4);
    const int q = r >> 2, pp = r & 3;
    c.vb0 = (g >> 1) * 2048 + (4 * (g & 1) + q) * 64 + (g & 1) * 32 + 8 * pp;
    c.vb1 = c.vb0 ^ 32;
    c.dstw = wave * 2048;
    c.slot = 0;
  }
  const unsigned hoff = h * 256;
  c.hoffn = hoff;
  const unsigned vX = r * (DM * 2) + g * 16;
  unsigned sot[NT];
#pragma unroll
  for (int t = 0; t < NT; ++t) sot[t] = (unsigned)((b * SQ + (t0 + t) * 16) * (DM * 2));
  const size_t kvoff = (size_t)(b * NH + h) * SK * DH * 2;

  bf16x8 X0[2][NT], X1[2][NT], X2[2][NT], X3[2][NT];
  srd_t sW = make_srd(prm.wq[0], DM * NH * DH * 2);
  srd_t sX = make_srd(prm.x[0], NB * SQ * DM * 2);
  issue_w(c, sW, hoff, 0, 0 * UNIT); issue_w(c, sW, hoff, 1, 1 * UNIT); issue_w(c, sW, hoff, 2, 2 * UNIT); issue_w(c, sW, hoff, 3, 3 * UNIT);
  {
    const int wp = blockIdx.x >> 8, ks = (blockIdx.x >> 1) & 127, hf = blockIdx.x & 1;
    const unsigned short* wo = (const unsigned short*)prm.wo[wp];
    for (int i = wave; i < 18; i += 4) {
      const int nt = hf * 18 + i;
      const unsigned short* src = wo + (size_t)(32 * ks + 4 * g) * DM + nt * 16 + r;
      u32x4 w;
      w[0] = src[0] | ((unsigned)src[DM] << 16);
      w[1] = src[2 * DM] | ((unsigned)src[3 * DM] << 16);
      w[2] = src[16 * DM] | ((unsigned)src[17 * DM] << 16);
      w[3] = src[18 * DM] | ((unsigned)src[19 * DM] << 16);
      *(u32x4*)(prm.ws + WS_WO + ((size_t)(wp * 36 + nt) * 128 + ks) * 1024 + lane * 16) = w;
    }
    if (wave == 0 && lane < 2) *(unsigned*)(prm.ws + WS_FLAG + (size_t)(blockIdx.x * 2 + lane) * 128) = 0u;
    asm volatile("s_waitcnt vmcnt(0)" ::: "memory");
  }
  load_x<NT, 0>(X0, vX, sX, sot); load_x<NT, 1>(X1, vX, sX, sot); load_x<NT, 2>(X2, vX, sX, sot); load_x<NT, 3>(X3, vX, sX, sot);

#pragma unroll 1
  for (int p = 0; p < 2; ++p) {
    c.sK = make_srd((const char*)(p ? prm.k[1] : prm.k[0]) + kvoff, SK * DH * 2);
    c.sV = make_srd((const char*)(p ? prm.v[1] : prm.v[0]) + kvoff, SK * DH * 2);
    c.sWn = make_srd(prm.wq[1], DM * NH * DH * 2);
    K1State<NT> st;
    {
      f32x4 A[8][NT];
      qstep<NT, 0>(A, X0, c, sW, hoff, vX, sX, sot);
      qstep<NT, 1>(A, X1, c, sW, hoff, vX, sX, sot);
      qstep<NT, 2>(A, X2, c, sW, hoff, vX, sX, sot);
      qstep<NT, 3>(A, X3, c, sW, hoff, vX, sX, sot);
      qstep<NT, 4>(A, X0, c, sW, hoff, vX, sX, sot);
      qstep<NT, 5>(A, X1, c, sW, hoff, vX, sX, sot);
      qstep<NT, 6>(A, X2, c, sW, hoff, vX, sX, sot);
      qstep<NT, 7>(A, X3, c, sW, hoff, vX, sX, sot);
      qstep<NT, 8>(A, X0, c, sW, hoff, vX, sX, sot);
#pragma unroll
      for (int s = 0; s < 4; ++s)
#pragma unroll
        for (int t = 0; t < NT; ++t) {
          const f32x4 a = A[2 * s][t] * QSCALE, bb = A[2 * s + 1][t] * QSCALE;
          auto r0 = __builtin_amdgcn_permlane32_swap(cvtpk(a[0], a[1]), cvtpk(bb[0], bb[1]), false, false);
          auto r1 = __builtin_amdgcn_permlane32_swap(cvtpk(a[2], a[3]), cvtpk(bb[2], bb[3]), false, false);
          auto q0 = __builtin_amdgcn_permlane16_swap(r0[0], r0[1], false, false);
          auto q1 = __builtin_amdgcn_permlane16_swap(r1[0], r1[1], false, false);
          const u32x4 w = {q0[0], q1[0], q0[1], q1[1]};
          st.Q[s][t] = __builtin_bit_cast(bf16x8, w);
        }
    }
    f32x4 SA[2][NT], SB[2][NT];
    wait_bar<12>();
    issue_kv(c, 4, prev_slot(c.slot));
    qk<NT>(SA, st, c, true);
    init_max<NT>(SA, st);
    c.slot = next_slot(c.slot);
#pragma unroll 1
    for (int j = 1; j < 15; j += 2) {
      wait_bar<12>();
      if (j <= 12) issue_kv(c, j + 4, prev_slot(c.slot)); else issue_w(c, c.sWn, c.hoffn, j - 13, prev_slot(c.slot));
      qk<NT>(SB, st, c, false);
      pv<NT>(SA, st, c);
      check_max<NT>(SB, st);
      c.slot = next_slot(c.slot);
      wait_bar<12>();
      if (j + 1 <= 12) issue_kv(c, j + 5, prev_slot(c.slot)); else issue_w(c, c.sWn, c.hoffn, j - 12, prev_slot(c.slot));
      qk<NT>(SA, st, c, false);
      pv<NT>(SB, st, c);
      check_max<NT>(SA, st);
      c.slot = next_slot(c.slot);
    }
    wait_bar<12>();
    issue_w(c, c.sWn, c.hoffn, 2, prev_slot(c.slot));
    qk<NT>(SB, st, c, false);
    pv<NT>(SA, st, c);
    check_max<NT>(SB, st);
    c.slot = next_slot(c.slot);
    wait_bar<12>();
    issue_w(c, c.sWn, c.hoffn, 3, prev_slot(c.slot));
    pv<NT>(SB, st, c);
    c.slot = next_slot(c.slot);
    sW = c.sWn;
    sX = make_srd(prm.x[1], NB * SQ * DM * 2);
    u32x4 W[4][NT];
#pragma unroll
    for (int t = 0; t < NT; ++t) {
      const float il = __builtin_amdgcn_rcpf(st.L[t][0]);
#pragma unroll
      for (int s = 0; s < 4; ++s) {
        const f32x4 a = st.O[2 * s][t] * il, bb = st.O[2 * s + 1][t] * il;
        W[s][t] = u32x4{cvtpk(a[0], a[1]), cvtpk(a[2], a[3]), cvtpk(bb[0], bb[1]), cvtpk(bb[2], bb[3])};
      }
    }
    load_x<NT, 0>(X0, vX, sX, sot); load_x<NT, 1>(X1, vX, sX, sot); load_x<NT, 2>(X2, vX, sX, sot); load_x<NT, 3>(X3, vX, sX, sot);
    asm volatile("" ::: "memory");
#pragma unroll
    for (int t = 0; t < NT; ++t)
#pragma unroll
      for (int s = 0; s < 4; ++s)
        *(u32x4*)(prm.ws + WS_O + ((size_t)(p * 144 + b * 9 + t0 + t) * 128 + h * 4 + s) * 1024 + lane * 16) = W[s][t];
  }
  asm volatile("s_waitcnt vmcnt(0)" : "+v"(X0[0][0]), "+v"(X1[0][0]), "+v"(X2[0][0]), "+v"(X3[0][0])::"memory");
}

__global__ __launch_bounds__(256, 2) void attn_kernel(K1Params prm) {
  const int wave = rfl(threadIdx.x >> 6), lane = threadIdx.x & 63;
  if (wave == 0) k1_body<3>(prm, 0, wave, lane);
  else k1_body<2>(prm, 1 + 2 * wave, wave, lane);
}

constexpr unsigned K2_STAGE = 36 * 1024, K2_LDS = 4 * K2_STAGE;

template <int MT, int NTL> struct K2Frag { bf16x8 o[MT], w[NTL]; };

template <int MT, int NTL> DI void k2_read(K2Frag<MT, NTL>& f, unsigned base, int m0, int n0) {
#pragma unroll
  for (int i = 0; i < MT; ++i) f.o[i] = lds_b128(base + (m0 + i) * 1024);
#pragma unroll
  for (int i = 0; i < NTL; ++i) f.w[i] = lds_b128(base + (9 + n0 + i) * 1024);
}
template <int MT, int NTL> DI void k2_mma(f32x4 (&acc)[MT][NTL], const K2Frag<MT, NTL>& f) {
#pragma unroll
  for (int i = 0; i < MT; ++i)
#pragma unroll
    for (int j = 0; j < NTL; ++j) acc[i][j] = mfma(f.w[j], f.o[i], acc[i][j]);
}
template <int CNT> DI void k2_issue(const unsigned (&so)[5], unsigned wave, unsigned voff, srd_t srd, int unit) {
  const unsigned d = (unsigned)(unit & 3) * K2_STAGE + wave * 1024;
#pragma unroll
  for (int i = 0; i < CNT; ++i) dma(d + i * 8192, voff, srd, so[i] + (unsigned)unit * 2048u);
}

template <int MT, int NTL, int M0, int N0, int KG, bool KEEPA>
DI void k2_epilogue(f32x4 (&acc)[MT][NTL], char* ws, unsigned short* out, int lane, int role, int p, int b, int ng) {
  constexpr int T = MT * NTL, TH = (T + 1) / 2;
  constexpr int KLO = KEEPA ? 0 : TH, KHI = KEEPA ? TH : T, GLO = KEEPA ? TH : 0, GHI = KEEPA ? T : TH;
#pragma unroll
  for (int q = GLO; q < GHI; ++q) {
    const int i = q / NTL, j = q % NTL;
    *(LAS f32x4*)(unsigned)(((M0 + i) * 9 + N0 + j) * 1024 + lane * 16) = acc[i][j];
  }
  asm volatile("s_waitcnt lgkmcnt(0)\n\ts_barrier" ::: "memory");
#pragma unroll
  for (int q = KLO; q < KHI; ++q) {
    const int i = q / NTL, j = q % NTL;
    acc[i][j] += *(LAS f32x4*)(unsigned)(((M0 + i) * 9 + N0 + j) * 1024 + lane * 16);
  }
  if constexpr (KG == 1) {
    srd_t sp = make_srd(ws + WS_PART + (size_t)blockIdx.x * 81 * 1024, 81 * 1024);
#pragma unroll
    for (int q = KLO; q < KHI; ++q) {
      const int i = q / NTL, j = q % NTL;
      __builtin_amdgcn_raw_buffer_store_b128(__builtin_bit_cast(u32x4, acc[i][j]), sp, lane * 16, ((M0 + i) * 9 + N0 + j) * 1024, 16);
    }
    asm volatile("s_waitcnt vmcnt(0)" ::: "memory");
    if (lane == 0)
      __hip_atomic_store((GAS unsigned*)(ws + WS_FLAG + (size_t)(blockIdx.x * 4 + role) * 128), 1u, __ATOMIC_RELAXED, __HIP_MEMORY_SCOPE_AGENT);
  } else {
    const unsigned partner = blockIdx.x ^ 2u;
    GAS unsigned* flag = (GAS unsigned*)(ws + WS_FLAG + (size_t)(partner * 4 + role) * 128);
    for (unsigned spins = 0; spins < (1u << 22); ++spins) {
      if (rfl(__hip_atomic_load(flag, __ATOMIC_RELAXED, __HIP_MEMORY_SCOPE_AGENT)) == 1u) break;
      __builtin_amdgcn_s_sleep(1);
    }
    __builtin_amdgcn_fence(__ATOMIC_ACQUIRE, "agent");
    srd_t sp = make_srd(ws + WS_PART + (size_t)partner * 81 * 1024, 81 * 1024);
    const int g = lane >> 4, r = lane & 15;
    u32x4 pr[KHI - KLO];
#pragma unroll
    for (int q = KLO; q < KHI; ++q) {
      const int i = q / NTL, j = q % NTL;
      pr[q - KLO] = __builtin_amdgcn_raw_buffer_load_b128(sp, lane * 16, ((M0 + i) * 9 + N0 + j) * 1024, 16);
    }
#pragma unroll
    for (int q = KLO; q < KHI; ++q) {
      const int i = q / NTL, j = q % NTL;
      const f32x4 v = acc[i][j] + __builtin_bit_cast(f32x4, pr[q - KLO]);
      const u32x2 w = {cvtpk(v[0], v[1]), cvtpk(v[2], v[3])};
      *(u32x2*)(out + (size_t)(b * SQ + (M0 + i) * 16 + r) * (2 * DM) + p * DM + ng * 144 + (N0 + j) * 16 + 4 * g) = w;
    }
  }
}

template <int MT, int NTL, int M0, int N0, int KG>
DI void k2_body(char* ws, unsigned short* out, const int wave, const int lane, const int role) {
  constexpr int CNT = KG == 0 ? 5 : 4;
  const int xcd = blockIdx.x & 7, idx = blockIdx.x >> 3;
  const int p = xcd >> 2, kh = (xcd >> 1) & 1, b = (xcd & 1) * 8 + (idx >> 2), ng = idx & 3;
  srd_t srd = make_srd(ws, (unsigned)(WS_O_BYTES + WS_WO_BYTES));
  unsigned so[5];
#pragma unroll
  for (int i = 0; i < 5; ++i) {
    const int pc = min(wave + 8 * i, 35), kg = pc / 18, rr = pc % 18;
    const unsigned tile = rr < 9 ? (unsigned)(p * 144 + b * 9 + rr) * 131072u : (unsigned)WS_WO + (unsigned)(p * 36 + ng * 9 + rr - 9) * 131072u;
    so[i] = tile + (unsigned)(kh * 64 + kg) * 1024u;
  }
  const unsigned voff = lane * 16;
  const unsigned rbase = KG * 18 * 1024 + lane * 16;
  f32x4 acc[MT][NTL];
#pragma unroll
  for (int i = 0; i < MT; ++i)
#pragma unroll
    for (int j = 0; j < NTL; ++j) acc[i][j] = f32x4{0.f, 0.f, 0.f, 0.f};
  K2Frag<MT, NTL> fa, fb;
  k2_issue<CNT>(so, wave, voff, srd, 0); k2_issue<CNT>(so, wave, voff, srd, 1); k2_issue<CNT>(so, wave, voff, srd, 2); k2_issue<CNT>(so, wave, voff, srd, 3);
  wait_bar<3 * CNT>();
  k2_read<MT, NTL>(fa, rbase, M0, N0);
#pragma unroll 1
  for (int s = 0; s < 28; s += 2) {
    wait_bar<2 * CNT>();
    k2_issue<CNT>(so, wave, voff, srd, s + 4);
    k2_read<MT, NTL>(fb, rbase + ((s + 1) & 3) * K2_STAGE, M0, N0);
    k2_mma<MT, NTL>(acc, fa);
    wait_bar<2 * CNT>();
    k2_issue<CNT>(so, wave, voff, srd, s + 5);
    k2_read<MT, NTL>(fa, rbase + ((s + 2) & 3) * K2_STAGE, M0, N0);
    k2_mma<MT, NTL>(acc, fb);
  }
  wait_bar<2 * CNT>();
  k2_read<MT, NTL>(fb, rbase + 1 * K2_STAGE, M0, N0);
  k2_mma<MT, NTL>(acc, fa);
  wait_bar<CNT>();
  k2_read<MT, NTL>(fa, rbase + 2 * K2_STAGE, M0, N0);
  k2_mma<MT, NTL>(acc, fb);
  wait_bar<0>();
  k2_read<MT, NTL>(fb, rbase + 3 * K2_STAGE, M0, N0);
  k2_mma<MT, NTL>(acc, fa);
  wait_bar<0>();
  k2_mma<MT, NTL>(acc, fb);
  if (kh == KG) k2_epilogue<MT, NTL, M0, N0, KG, true>(acc, ws, out, lane, role, p, b, ng);
  else          k2_epilogue<MT, NTL, M0, N0, KG, false>(acc, ws, out, lane, role, p, b, ng);
}

__global__ __launch_bounds__(512, 2) void oproj_kernel(char* ws, unsigned short* out) {
  const int wave = rfl(threadIdx.x >> 6), lane = threadIdx.x & 63;
  switch (wave) {
    case 0: k2_body<5, 5, 0, 0, 0>(ws, out, wave, lane, 0); break;
    case 1: k2_body<5, 4, 0, 5, 0>(ws, out, wave, lane, 1); break;
    case 2: k2_body<4, 5, 5, 0, 0>(ws, out, wave, lane, 2); break;
    case 3: k2_body<4, 4, 5, 5, 0>(ws, out, wave, lane, 3); break;
    case 4: k2_body<4, 4, 5, 5, 1>(ws, out, wave, lane, 3); break;
    case 5: k2_body<4, 5, 5, 0, 1>(ws, out, wave, lane, 2); break;
    case 6: k2_body<5, 4, 0, 5, 1>(ws, out, wave, lane, 1); break;
    default: k2_body<5, 5, 0, 0, 1>(ws, out, wave, lane, 0); break;
  }
}

extern "C" void kernel_launch(void* const* d_in, const int* in_sizes, int n_in, void* d_out, int out_size, void* d_ws, size_t ws_size,
                              hipStream_t stream) {
  (void)in_sizes; (void)n_in; (void)out_size; (void)ws_size;
  hipFuncSetAttribute((const void*)attn_kernel, hipFuncAttributeMaxDynamicSharedMemorySize, (int)K1_LDS);
  hipFuncSetAttribute((const void*)oproj_kernel, hipFuncAttributeMaxDynamicSharedMemorySize, (int)K2_LDS);
  K1Params prm;
  for (int p = 0; p < 2; ++p) {
    prm.x[p] = d_in[5 * p + 0]; prm.k[p] = d_in[5 * p + 1]; prm.v[p] = d_in[5 * p + 2];
    prm.wq[p] = d_in[5 * p + 3]; prm.wo[p] = d_in[5 * p + 4];
  }
  prm.ws = (char*)d_ws;
  hipLaunchKernelGGL(attn_kernel, dim3(512), dim3(256), K1_LDS, stream, prm);
  hipLaunchKernelGGL(oproj_kernel, dim3(256), dim3(512), K2_LDS, stream, (char*)d_ws, (unsigned short*)d_out);
}
```

```cpp
#include <hip/hip_runtime.h>
#include <stdint.h>
#include <utility>

typedef __bf16 bf16x8 __attribute__((ext_vector_type(8)));
typedef __bf16 bf16x4 __attribute__((ext_vector_type(4)));
typedef __bf16 bf16x2 __attribute__((ext_vector_type(2)));
typedef float f32x4 __attribute__((ext_vector_type(4)));
typedef float f32x2 __attribute__((ext_vector_type(2)));
typedef unsigned u32x4 __attribute__((ext_vector_type(4)));
typedef unsigned u32x2 __attribute__((ext_vector_type(2)));
typedef __amdgpu_buffer_rsrc_t srd_t;
#define LAS __attribute__((address_space(3)))
#define GAS __attribute__((address_space(1)))
#define DI __device__ __forceinline__

constexpr int SQ = 144, DM = 576, NH = 32, SK = 512, DH = 128, NB = 16;
constexpr float QSCALE = 0.088388347648318440f * 1.4426950408889634f;
constexpr float THR = 8.0f;

constexpr size_t WS_REQ = 0;
constexpr size_t WS_RSP = 256 * 128;
constexpr size_t WS_PART = 512 * 128;
constexpr size_t WS_PART_BYTES = (size_t)256 * 162 * 1024;

DI unsigned rfl(unsigned x) { return __builtin_amdgcn_readfirstlane(x); }
DI int lane_id() { int l; asm volatile("v_mbcnt_lo_u32_b32 %0, -1, 0\n\tv_mbcnt_hi_u32_b32 %0, -1, %0" : "=v"(l)); return l; }
DI srd_t make_srd(const void* p, unsigned bytes) { return __builtin_amdgcn_make_buffer_rsrc((void*)p, (short)0, (int)bytes, 0x00020000); }
DI unsigned cvtpk(float lo, float hi) { f32x2 v = {lo, hi}; bf16x2 b = __builtin_convertvector(v, bf16x2); return __builtin_bit_cast(unsigned, b); }
DI f32x4 mfma(bf16x8 a, bf16x8 b, f32x4 c) { return __builtin_amdgcn_mfma_f32_16x16x32_bf16(a, b, c, 0, 0, 0); }
DI void mfma_acc(f32x4& c, bf16x8 a, bf16x8 b) { asm volatile("v_mfma_f32_16x16x32_bf16 %0, %1, %2, %0" : "+v"(c) : "v"(a), "v"(b)); }
DI bf16x8 lds_b128(unsigned addr) { return *(LAS bf16x8*)addr; }
DI bf16x8 lds_tr2(unsigned a0, unsigned a1) {
  bf16x4 lo = __builtin_amdgcn_ds_read_tr16_b64_v4bf16((LAS bf16x4*)a0);
  bf16x4 hi = __builtin_amdgcn_ds_read_tr16_b64_v4bf16((LAS bf16x4*)a1);
  return __builtin_shufflevector(lo, hi, 0, 1, 2, 3, 4, 5, 6, 7);
}
DI unsigned mulpk(float a, float b, float f) {
  float x = a * f, y = b * f; asm volatile("" : "+v"(x)); asm volatile("" : "+v"(y)); return cvtpk(x, y);
}
DI void pin4(unsigned& a, unsigned& b, unsigned& c, unsigned& d) { asm volatile("" : "+v"(a), "+v"(b), "+v"(c), "+v"(d)); }
DI void pin2(unsigned& a, unsigned& b) { asm volatile("" : "+v"(a), "+v"(b)); }
DI void keep(unsigned a) { asm volatile("" ::"v"(a)); }
DI void pin1(unsigned& a) { asm volatile("" : "+v"(a)); }
template <class T> DI void pin_s1(T& a) { asm volatile("" : "+s"(a)); }
DI void pin_s(unsigned long long& a) { asm volatile("" : "+s"(a)); }
DI int imax3(int a, int b, int c) { return max(max(a, b), c); }

struct K1Params {
  const void* x[2]; const void* k[2]; const void* v[2]; const void* wq[2]; const void* wo[2];
  char* ws; unsigned short* out;
};

constexpr unsigned WREG = 65536, XREG = WREG + 6 * 8192, OREG = XREG + 3 * 12288, K1_LDS = OREG + 12288;
#define SB() __builtin_amdgcn_sched_barrier(0)
template <int N> DI void vm_bar() { asm volatile("s_waitcnt vmcnt(%0)\n\ts_barrier" ::"i"(N) : "memory"); }
DI void bar() { asm volatile("s_barrier" ::: "memory"); }
DI void vm_wait0() { asm volatile("s_waitcnt vmcnt(0)" ::: "memory"); }
template <int C> DI void lg_wait() { __builtin_amdgcn_s_waitcnt(0xC07F | (C << 8)); }
template <int I> using IC = std::integral_constant<int, I>;
template <class F, int... Is> DI void static_for_(F&& f, std::integer_sequence<int, Is...>) { (f(IC<Is>{}), ...); }
template <int N, class F> DI void static_for(F&& f) { static_for_(f, std::make_integer_sequence<int, N>{}); }

constexpr int RW = 4;
constexpr int NRES = 4;
#define GTAB 6, 12, 15, 15, 8, 8, 8, 8, 8, 8, 8, 8, 8, 9, 9, 6
constexpr int gstart(int pi) {
  constexpr int t[16] = {GTAB};
  int g = 0;
  for (int i = 0; i < pi; ++i) g += t[i];
  return g;
}
static_assert(gstart(16) == 144, "");
constexpr int pi_of(int g) { int it = 0; while (g < 0) { g += 144; --it; } while (g >= 144) { g -= 144; ++it; } int pi = 15; while (gstart(pi) > g) --pi; return it * 16 + pi; }
constexpr int needW(int s) { return pi_of(8 * s - RW); }
constexpr bool w_ok();
constexpr int issueW(int s) { return pi_of(8 * (s - 6) + 7) + 1; }
constexpr bool w_ok() { for (int s = 6; s < 72; ++s) if (issueW(s) >= needW(s)) return false; return gstart(1) > 3 && gstart(2) + 12 <= 16 * NRES - 8; }
static_assert(w_ok(), "Wq ring / x schedule");
constexpr int l_count(int pi) {
  int cnt = 0;
  for (int s = 0; s < 6; ++s) { cnt += 8; if (needW(s) <= pi) cnt = 0; }
  for (int i = 0; i < pi; ++i) {
    for (int s = 6; s < 72; ++s) if (issueW(s) == i) { cnt += 8; if (needW(s) <= pi) cnt = 0; }
    if (i >= 16 && i < 80) { cnt += 8; if (i + 2 <= pi) cnt = 0; }
    if (i >= 14 && i < 78) { cnt += 8; if (i + 3 <= pi) cnt = 0; }
  }
  return cnt > 63 ? 63 : cnt;
}
struct Loader {
  srd_t sW, sK, sV;
  unsigned vw[4], v0, v1, row, col; int rk;
};
template <int S> DI void issue_w(Loader& l) {
  if constexpr (S == 0) l.col += 256;
  unsigned tmp;
  asm volatile("s_mov_b32 m0, %8\n\ts_add_u32 %0, %6, %7\n\tbuffer_load_dwordx4 %1, %5, %0 offen nt lds\n\tbuffer_load_dwordx4 %2, %5, %0 offen offset:1024 nt lds\n\t"
               "buffer_load_dwordx4 %3, %5, %0 offen offset:2048 nt lds\n\tbuffer_load_dwordx4 %4, %5, %0 offen offset:3072 nt lds\n\t"
               "s_mov_b32 m0, %9\n\ts_add_u32 %0, %0, 0x8000\n\tbuffer_load_dwordx4 %1, %5, %0 offen nt lds\n\tbuffer_load_dwordx4 %2, %5, %0 offen offset:1024 nt lds\n\t"
               "buffer_load_dwordx4 %3, %5, %0 offen offset:2048 nt lds\n\tbuffer_load_dwordx4 %4, %5, %0 offen offset:3072 nt lds"
               : "=&s"(tmp) : "v"(l.vw[0]), "v"(l.vw[1]), "v"(l.vw[2]), "v"(l.vw[3]), "s"(l.sW), "s"(l.row), "s"(l.col),
                 "i"(WREG + (S % 6) * 8192), "i"(WREG + (S % 6) * 8192 + 4096) : "memory", "scc");
  l.row += 32 * 8192; if (l.row == 18 * 32 * 8192) l.row = 0;
}
constexpr int w_at(int pi, int k) { for (int s = 6; s < 72; ++s) if (issueW(s) == pi && k-- == 0) return s; return -1; }
template <int PI> DI void issue_w_interval(Loader& l) {
  static_assert(w_at(PI, 3) < 0, "");
  if constexpr (w_at(PI, 0) >= 0) issue_w<w_at(PI, 0) % 18>(l);
  if constexpr (w_at(PI, 1) >= 0) issue_w<w_at(PI, 1) % 18>(l);
  if constexpr (w_at(PI, 2) >= 0) issue_w<w_at(PI, 2) % 18>(l);
}
template <int LDSOFF> DI void issue_8k(unsigned v0, unsigned v1, srd_t srd, int e) {
  unsigned tmp;
  asm volatile("s_mov_b32 m0, %5\n\ts_lshl_b32 %0, %4, 13\n\tbuffer_load_dwordx4 %1, %3, %0 offen nt lds\n\tbuffer_load_dwordx4 %2, %3, %0 offen offset:1024 nt lds\n\t"
               "buffer_load_dwordx4 %1, %3, %0 offen offset:2048 nt lds\n\tbuffer_load_dwordx4 %2, %3, %0 offen offset:3072 nt lds\n\t"
               "s_mov_b32 m0, %6\n\ts_add_u32 %0, %0, 0x1000\n\tbuffer_load_dwordx4 %1, %3, %0 offen nt lds\n\tbuffer_load_dwordx4 %2, %3, %0 offen offset:1024 nt lds\n\t"
               "buffer_load_dwordx4 %1, %3, %0 offen offset:2048 nt lds\n\tbuffer_load_dwordx4 %2, %3, %0 offen offset:3072 nt lds"
               : "=&s"(tmp) : "v"(v0), "v"(v1), "s"(srd), "s"(e & 63), "i"(LDSOFF), "i"(LDSOFF + 4096) : "memory", "scc");
}
template <int KI> DI void issue_unit(Loader& l) {
  constexpr int SLOT = (KI + 3) & 3;
  if constexpr (KI >= 2 && KI < 66) issue_8k<SLOT * 16384 + 8192>(l.v0, l.v1, l.sV, (((KI - 2) & 48) | ((KI - 2 + l.rk) & 15)));
  if constexpr (KI < 64) issue_8k<SLOT * 16384>(l.v0, l.v1, l.sK, ((KI & 48) | ((KI + l.rk) & 15)));
}

DI bf16x8 rd_tr(unsigned a0, unsigned a1, int off, int cc) { const unsigned b = ((cc & 1) ? a1 : a0) + off + (cc >> 1) * 256; return lds_tr2(b, b + 4096); }
DI bf16x8 rd_kf(unsigned ka, int slot, int i) { return lds_b128(ka + slot * 16384 + (i >> 2) * 4096 + (i & 3) * 256); }

DI float fmx(float a, float b) { return __builtin_elementwise_maximum(a, b); }
DI float xmax(float r) {
  auto a = __builtin_amdgcn_permlane32_swap(__float_as_uint(r), __float_as_uint(r), false, false);
  r = fmx(__uint_as_float(a[0]), __uint_as_float(a[1]));
  auto b = __builtin_amdgcn_permlane16_swap(__float_as_uint(r), __float_as_uint(r), false, false);
  return fmx(__uint_as_float(b[0]), __uint_as_float(b[1]));
}
template <int NT> DI float tile_max(const f32x4 (&S)[2][NT], int t) {
  float r = fmx(fmx(S[0][t][0], S[0][t][1]), S[0][t][2]);
  r = fmx(fmx(r, S[0][t][3]), S[1][t][0]);
  r = fmx(fmx(r, S[1][t][1]), S[1][t][2]);
  return xmax(fmx(r, S[1][t][3]));
}
template <int NT> DI void sub_all(f32x4 (&S)[2][NT], int t, float r) {
#pragma unroll
  for (int e = 0; e < 8; ++e) { float v = S[e >> 2][t][e & 3] - r; asm volatile("" : "+v"(v)); S[e >> 2][t][e & 3] = v; }
}
template <int NT> struct Attn {
  f32x4 O[8][NT], L[NT], NM[NT];
  bf16x8 Q[4][NT];
  bf16x8 kf[8], ones;
  bf16x4 vh[16];
  f32x4 OF[8][NT], LF[NT];
  u32x4 P[NT], Z[3];
  int mx; unsigned long long over;
  unsigned ka, va0, va1, qa, w0, w1, wd;
};
template <int NT> DI void rescale(f32x4 (&S)[2][NT], Attn<NT>& a) {
  asm volatile("s_nop 7\n\ts_nop 1" ::: "memory"); SB();
#pragma unroll
  for (int t = 0; t < NT; ++t) {
    const float dl = ceilf(fmaxf(tile_max<NT>(S, t), 0.f));
    const float f = __builtin_amdgcn_exp2f(-dl);
    S[0][t] -= dl; S[1][t] -= dl;
    a.NM[t] -= dl;
    a.L[t] *= f;
#pragma unroll
    for (int cc = 0; cc < 8; ++cc) a.O[cc][t] *= f;
#pragma unroll
    for (int e = 0; e < 4; ++e) a.P[t][e] = cvtpk(__uint_as_float(a.P[t][e] << 16) * f, __uint_as_float(a.P[t][e] & 0xffff0000u) * f);
  }
  SB(); asm volatile("s_nop 1" ::: "memory");
}

#define CHAIN2 1
#define CHAIN3 1
template <int NT> constexpr bool CHAIN = NT == 2 ? CHAIN2 : CHAIN3;
template <int NT> constexpr int qk_frag(int m) { return CHAIN<NT> ? 4 * (m / (4 * NT)) + (m & 3) : m / NT; }
template <int NT> constexpr int qk_tile(int m) { return CHAIN<NT> ? (m % (4 * NT)) >> 2 : m % NT; }
template <int NT> constexpr int frag_of(int g) { return g < NT ? -1 : g < 9 * NT ? g / NT - 1 : 8 + qk_frag<NT>(g - 9 * NT); }
template <int NT> struct Prog { char a[17 * NT][4]; int x[17 * NT][4]; };
template <int NT> constexpr Prog<NT> make_prog() {
  const char* p2[34] = {"mm", "mm", "hK", "Bh", "we", "hh", "we", "hh", "we", "hh", "we", "hh", "we", "dd", "we", "e", "we", "dd",
                        "we", "e", "we", "dd", "we", "e", "we", "dd", "we", "e", "whcc", "hmm", "whcc", "hmm", "hcc", "hcc"};
  const char* c2[34] = {"mm", "mm", "hK", "Bh", "we", "hh", "we", "hh", "we", "hh", "we", "hh", "we", "dd", "we", "dd", "e", "we",
                        "e", "we", "dd", "e", "dd", "e", "e", "we", "e", "we", "hcc", "hmm", "hcc", "hmm", "hcc", "hcc"};
  const char* p3[51] = {"mm", "mm", "mm", "hK", "Bh", "we", "e", "we", "hh", "e", "we", "hh", "e", "we", "hh", "e", "we", "hh", "e", "we", "e", "e", "we", "dd", "e", "we", "d",
                        "cc", "we", "dc", "cc", "we", "dc", "cc", "we", "d", "e", "we", "d", "e", "we", "d", "e", "wmm", "hh", "cc", "wmm", "hh", "cc", "mm", "hh"};
  const char* c3[51] = {"mm", "mm", "mm", "hK", "Bh", "we", "e", "we", "hh", "e", "we", "hh", "e", "we", "hh", "e", "we", "hh", "e", "we", "e", "e", "we", "dd", "e", "dd", "we",
                        "cc", "wcc", "cc", "cc", "e", "e", "e", "dd", "e", "dd", "e", "we", "e", "we", "mm", "mm", "mm", "cc", "cc", "hh", "", "hh", "", "hh"};
  Prog<NT> p = {};
  const char* const* pp = NT == 2 ? (CHAIN<NT> ? c2 : p2) : (CHAIN<NT> ? c3 : p3);
  int e = 0, c = 0, m = 2 * NT, h = 6, d = 0, nds = 6, at[16] = {2, 4, 6};
  for (int g = 0; g < 17 * NT; ++g) {
    const char* t = pp[g];
    for (int k = 0; t[k]; ++k) {
      p.a[g][k] = t[k];
      if (t[k] == 'e') p.x[g][k] = e++;
      if (t[k] == 'c') p.x[g][k] = c++;
      if (t[k] == 'm') { p.x[g][k] = m++; if (m == 4 * NT) m = 0; }
      if (t[k] == 'h') { p.x[g][k] = h; ++nds; if (h < 16) at[h >> 1] = nds; ++h; }
      if (t[k] == 'd') { p.x[g][k] = d; at[8 + d++] = ++nds; }
      if (t[k] == 'w') {
        int need = 0; bool bad = false;
        for (int g2 = g + 1; g2 < 17 * NT; ++g2) { const int u = frag_of<NT>(g2); if (!at[u]) bad = true; if (at[u] > need) need = at[u]; if (pp[g2][0] == 'w') break; }
        p.x[g][k] = bad ? -1 : nds - need;
      }
    }
  }
  bool ok = e == 8 * NT && c == 4 * NT && m == 2 * NT && h == 22 && d == 8;
  for (int g = 0; g < 17 * NT; ++g) for (int k = 0; k < 4; ++k) if (p.x[g][k] < 0) ok = false;
  p.a[0][3] = ok;
  return p;
}
static_assert(make_prog<2>().a[0][3] == 1 && make_prog<3>().a[0][3] == 1, "program");

template <int NT> DI void do_exp(f32x4 (&S)[2][NT], int e) { const int t = e >> 3, k = e & 7;
  float v = __builtin_amdgcn_exp2f(S[k >> 2][t][k & 3]);
  asm volatile("" : "+v"(v)); S[k >> 2][t][k & 3] = v; }
template <int NT> DI void do_cvt(Attn<NT>& a, f32x4 (&S)[2][NT], int c) {
  const int t = c >> 2, k = c & 3; unsigned v = cvtpk(S[k >> 1][t][2 * (k & 1)], S[k >> 1][t][2 * (k & 1) + 1]); asm volatile("" : "+v"(v)); a.P[t][k] = v;
}
template <int NT> DI void do_max(Attn<NT>& a, f32x4 (&S)[2][NT], int m) {
  const f32x4 v = S[m / (2 * NT)][(m % (2 * NT)) >> 1]; const int x = __float_as_int(v[2 * (m & 1)]), y = __float_as_int(v[2 * (m & 1) + 1]);
  a.mx = m == 0 ? max(x, y) : imax3(a.mx, x, y);
  if (m & 1) asm volatile("" : "+v"(a.mx));
}
template <int NT> DI void rd_vh(Attn<NT>& a, int slot, int h) {
  const int cc = h >> 1; a.vh[h] = __builtin_amdgcn_ds_read_tr16_b64_v4bf16((LAS bf16x4*)(((cc & 1) ? a.va1 : a.va0) + slot * 16384 + (cc >> 1) * 256 + (h & 1) * 4096));
}

template <int NT> DI void o_keep(Attn<NT>& a) {
#pragma unroll
  for (int t = 0; t < NT; ++t) {
    a.LF[t][0] = __builtin_amdgcn_rcpf(a.L[t][0]);
#pragma unroll
    for (int cc = 0; cc < 8; ++cc) a.OF[cc][t] = a.O[cc][t];
  }
}
template <int NT> DI unsigned o_word(Attn<NT>& a, int i, int e) {
  const f32x4 u = a.OF[2 * (i & 3) + (e >> 1)][i >> 2];
  unsigned z = mulpk(u[2 * (e & 1)], u[2 * (e & 1) + 1], a.LF[i >> 2][0]); asm volatile("" : "+v"(z)); return z;
}
template <int POS> DI void o_gap(Attn<3>& a) {
  constexpr int qw = (POS >> 1) - 1;
  if constexpr (POS == 0) { a.w0 = a.qa + a.wd; asm volatile("" : "+v"(a.w0)); }
  if constexpr ((POS & 1) == 0 && qw >= 0 && qw < 24 && (qw & 1)) { *(LAS u32x4*)(a.w0 + (((qw % 6) >> 1) * 4 + qw / 6) * 1024) = a.Z[0]; SB(); }
  constexpr int q = POS >> 1, c = 2 * (q / 6) + (q & 1), t = (q % 6) >> 1;
  if constexpr (q < 24) {
    if constexpr ((POS & 1) == 0) {
      const f32x4 u = a.OF[c][t]; const float il = a.LF[t][0]; float x = u[0] * il, y = u[1] * il, z = u[2] * il, w = u[3] * il;
      asm volatile("" : "+v"(x), "+v"(y), "+v"(z), "+v"(w)); a.OF[c][t] = f32x4{x, y, z, w};
    } else {
      const f32x4 u = a.OF[c][t]; unsigned lo = cvtpk(u[0], u[1]), hi = cvtpk(u[2], u[3]); asm volatile("" : "+v"(lo), "+v"(hi));
      if constexpr ((q & 1) == 0) a.Z[0] = u32x4{lo, hi, 0, 0}; else { a.Z[0][2] = lo; a.Z[0][3] = hi; }
    }
  }
}
template <int NT, int N, int MODE> DI void interval(Attn<NT>& a, f32x4 (&So)[2][NT], f32x4 (&Sn)[2][NT]) {
  constexpr Prog<NT> pr = make_prog<NT>();
  constexpr bool HINT = MODE == 0 && N != 15 && !(NT == 3 && N <= 1);
  const f32x4 zero = {0.f, 0.f, 0.f, 0.f};
  static_for<17 * NT>([&](auto ic) {
    constexpr int g = decltype(ic)::value, t = g % NT, cc = g / NT - 1, m = g - 9 * NT;
    constexpr bool PV = g < 9 * NT;
    if constexpr (PV ? MODE != 1 : MODE != 2) {
      if constexpr (PV) {
        const bf16x8 pb = __builtin_bit_cast(bf16x8, a.P[t]);
        if constexpr (cc < 0) { if constexpr (N <= 1) a.L[t] = mfma(a.ones, pb, N == 1 ? zero : a.L[t]); else mfma_acc(a.L[t], a.ones, pb); }
        else {
          const bf16x8 vf = __builtin_shufflevector(a.vh[2 * cc], a.vh[2 * cc + 1], 0, 1, 2, 3, 4, 5, 6, 7);
          if constexpr (N == 1) a.O[cc][t] = mfma(vf, pb, zero);
          else if constexpr (N == 0 && (NT == 2 || MODE == 2)) a.O[cc][t] = mfma(vf, pb, a.O[cc][t]);
          else mfma_acc(a.O[cc][t], vf, pb);
        }
      } else {
        constexpr int i = qk_frag<NT>(m), tq = qk_tile<NT>(m);
        if constexpr ((i & 3) == 0) Sn[i >> 2][tq] = mfma(a.kf[i], a.Q[0][tq], N == 15 ? zero : a.NM[tq]);
        else mfma_acc(Sn[i >> 2][tq], a.kf[i], a.Q[i & 3][tq]);
      }
      SB();
    }
    if constexpr (MODE == 0 && N == 15 && cc >= 0 && PV && t < NT - 1) {
      constexpr int qi = cc * (NT - 1) + t;
      if constexpr (qi < 4 * NT) { a.Q[qi & 3][qi >> 2] = lds_b128(a.qa + qi * 1024); SB(); }
    }
    static_for<4>([&](auto kc) {
      constexpr char ch = pr.a[g][decltype(kc)::value]; constexpr int x = pr.x[g][decltype(kc)::value];
      if constexpr (ch == 'w' && HINT) lg_wait<x>();
      if constexpr (ch == 'h' && x < 16 && MODE != 1) rd_vh<NT>(a, N & 3, x);
      if constexpr (ch == 'h' && x >= 16 && MODE != 2) rd_vh<NT>(a, (N + 1) & 3, x - 16);
      if constexpr (ch == 'd' && MODE != 2) a.kf[x] = rd_kf(a.ka, N & 3, x);
      if constexpr (MODE == 0) {
        if constexpr (ch == 'e') do_exp<NT>(So, x);
        if constexpr (ch == 'c') do_cvt<NT>(a, So, x);
        if constexpr (ch == 'm' && PV && N != 0) do_max<NT>(a, So, x);
        if constexpr (ch == 'm' && !PV && N != 15) do_max<NT>(a, Sn, x);
        if constexpr (ch == 'K' && N != 0) { a.over = __builtin_amdgcn_ballot_w64(a.mx > __float_as_int(THR)); pin_s(a.over); }
        if constexpr (ch == 'B') {
          if constexpr (N == 0) {
#pragma unroll
            for (int tt = 0; tt < NT; ++tt) { const float r = tile_max<NT>(So, tt); sub_all<NT>(So, tt, r); a.NM[tt] = f32x4{-r, -r, -r, -r}; }
          } else if (__builtin_expect(a.over != 0ull, 0)) rescale<NT>(So, a);
          pin4(a.ka, a.va0, a.va1, a.qa);
        }
      }
      if constexpr (ch > 1) SB();
    });
    if constexpr (N == 0 && g == 9 * NT - 1) o_keep<NT>(a);
    if constexpr (MODE == 0) {
      if constexpr (NT == 2) {
        if constexpr (N >= 3 && N <= 10 && g == 1) *(LAS u32x4*)((N < 7 ? a.w0 : a.w1) + ((N - 3) & 3) * 1024) = a.Z[(N - 3) % 3];
        if constexpr (N >= 1 && N <= 8 && g >= 13 && g <= 25 && (g & 3) == 1) a.Z[(N - 1) % 3][(g - 13) >> 2] = o_word<NT>(a, N - 1, (g - 13) >> 2);
      } else {
        if constexpr (N == 0 && !PV) o_gap<m>(a);
        if constexpr (N == 1 && PV) o_gap<24 + g>(a);
      }
    }
    SB();
  });
}

struct Item { int p, b, h0, rot; };
DI Item item_of() { const int xcd = blockIdx.x & 7, idx = blockIdx.x >> 3; Item it; it.p = xcd >> 2; it.b = (xcd & 1) * 8 + (idx >> 2); it.h0 = ((xcd >> 1) & 1) * 16 + (idx & 3) * 4;
  it.rot = 2 * (idx & 3);
  return it; }

DI void w_setup(Loader& l, const K1Params& prm, int lane) {
  const Item it = item_of();
  l.sW = make_srd(it.p ? prm.wq[1] : prm.wq[0], DM * NH * DH * 2);
#pragma unroll
  for (int i = 0; i < 4; ++i) l.vw[i] = (8 * i + ((lane >> 2) & 3)) * 8192 + (lane >> 4) * 64 + (((lane & 3) ^ ((i & 1) << 1)) << 4) - 1024 * i;
  l.row = it.rot * (64 * 8192); l.col = it.h0 * 256 - 256;
}
DI void touch(unsigned& sink, unsigned voff, srd_t srd) { asm volatile("buffer_load_dword %0, %1, %2, 0 offen" : "+v"(sink) : "v"(voff), "s"(srd) : "memory"); }

constexpr unsigned OT = 49152, X8 = XREG + 8 * 4096;
constexpr unsigned long long SLOT_TAB = 0x965408721ull;
DI unsigned sub_slot(int i) { return (unsigned)((SLOT_TAB >> (4 * i)) & 15u) << 14; }
DI unsigned p2_trbase(int lane) { const int g = lane >> 4; return (g >> 1) * 1024 + (4 * (g & 1) + ((lane & 15) >> 2)) * 64 + (g & 1) * 32 + 8 * (lane & 3); }
DI bf16x8 cat(bf16x4 lo, bf16x4 hi) { return __builtin_shufflevector(lo, hi, 0, 1, 2, 3, 4, 5, 6, 7); }
DI unsigned park0(int t) { return WREG + t * 4096; }
DI unsigned park1(int t) { return t == 7 ? X8 : (t & 1) ? OREG + (t >> 1) * 4096 : WREG + 32768 + (t >> 1) * 4096; }
struct P2Src { unsigned v[4]; };
DI void p2_issue4(unsigned dst, const P2Src& v, srd_t sw, unsigned so) {
  asm volatile("s_mov_b32 m0, %0\n\ts_nop 0\n\tbuffer_load_dwordx4 %1, %5, %6 offen lds\n\tbuffer_load_dwordx4 %2, %5, %6 offen offset:1024 lds\n\t"
               "buffer_load_dwordx4 %3, %5, %6 offen offset:2048 lds\n\tbuffer_load_dwordx4 %4, %5, %6 offen offset:3072 lds"
               ::"s"(dst), "v"(v.v[0]), "v"(v.v[1]), "v"(v.v[2]), "v"(v.v[3]), "s"(sw), "s"(so) : "memory");
}
DI P2Src p2_src(int lane) { const int vr = (lane >> 2) & 7; const unsigned b = vr * (DM * 2) + (lane >> 5) * 64 + (((lane & 3) ^ ((vr >> 2) << 1)) << 4);
  return P2Src{{b, b + 8 * DM * 2 - 1024, b + 16 * DM * 2 - 2048, b + 24 * DM * 2 - 3072}}; }
DI unsigned p2_so(const Item& it, int level, int col) { return (unsigned)((it.h0 + level) * 128) * (DM * 2) + col * 128; }

DI unsigned member(int m) { return (blockIdx.x & ~26u) | ((m >> 2) & 1) << 1 | (m & 3) << 3; }
DI unsigned my_rank() { return ((blockIdx.x >> 1) & 1) * 4 + ((blockIdx.x >> 3) & 3); }
DI GAS unsigned* hs_word(char* ws, size_t base, unsigned wg, unsigned i) { return (GAS unsigned*)(ws + base + (size_t)wg * 128 + i * 4); }
DI unsigned ld_agent(GAS unsigned* p) { return __hip_atomic_load(p, __ATOMIC_RELAXED, __HIP_MEMORY_SCOPE_AGENT); }
DI void st_agent(GAS unsigned* p, unsigned v) { __hip_atomic_store(p, v, __ATOMIC_RELAXED, __HIP_MEMORY_SCOPE_AGENT); }
DI void hs_request(char* ws, int lane) { if (lane < 8) st_agent(hs_word(ws, WS_REQ, member(lane), my_rank()), ld_agent(hs_word(ws, WS_RSP, blockIdx.x, lane)) + 1u); }
DI void reduce_out(const K1Params& prm, const int wave, const int lane) {
  const Item it = item_of();
  GAS unsigned* const inq = hs_word(prm.ws, WS_REQ, blockIdx.x, lane & 7);
  unsigned q = 0, want = 0;
  if (wave == 7) { q = ld_agent(inq); want = ld_agent(hs_word(prm.ws, WS_REQ, member(lane), my_rank())); }
  vm_bar<0>();
  GAS unsigned* const outr = hs_word(prm.ws, WS_RSP, member(lane), my_rank());
  if (wave == 7) {
    GAS unsigned* const inr = hs_word(prm.ws, WS_RSP, blockIdx.x, lane & 7);
    if (lane < 8) st_agent(outr, q);
#pragma unroll 1
    for (unsigned spins = 0; spins < (1u << 20); ++spins) {
      if (__builtin_amdgcn_ballot_w64(ld_agent(inr) != want) == 0ull) break;
      if ((spins & 7) == 7) { const unsigned q2 = ld_agent(inq); if (q2 != q && lane < 8) st_agent(outr, q2); q = q2; }
    }
  }
  bar();
  if (wave == 7) { const unsigned q2 = ld_agent(inq); if (q2 != q && lane < 8) st_agent(outr, q2); }
  __builtin_amdgcn_fence(__ATOMIC_ACQUIRE, "wavefront");
  const srd_t sp = make_srd(prm.ws + WS_PART, (unsigned)WS_PART_BYTES), so = make_srd(prm.out, NB * SQ * 2 * DM * 2);
  const int slot = my_rank() * 8 + wave;
  u32x4 v[3][8];
#pragma unroll
  for (int k = 0; k < 3; ++k)
#pragma unroll
    for (int m = 0; m < 8; ++m) v[k][m] = __builtin_amdgcn_raw_buffer_load_b128(sp, (member(m) * 162 + slot + 64 * k) * 1024 + lane * 16 + (slot + 64 * k < 162 ? 0 : 1u << 30), 0, 16);
#pragma unroll
  for (int k = 0; k < 3; ++k) {
    const int pc = slot + 64 * k, col = pc / 18, t = (pc % 18) >> 1, half = pc & 1;
    float a[8] = {0.f, 0.f, 0.f, 0.f, 0.f, 0.f, 0.f, 0.f};
#pragma unroll
    for (int m = 0; m < 8; ++m)
#pragma unroll
      for (int e = 0; e < 4; ++e) { a[2 * e] += __uint_as_float(v[k][m][e] << 16); a[2 * e + 1] += __uint_as_float(v[k][m][e] & 0xffff0000u); }
    const int l0 = t < 8 ? lane : 2 * (lane & 31);
    const unsigned ov = (unsigned)(it.b * SQ + t * 16 + (l0 & 15)) * (2 * DM * 2) + (it.p * DM + col * 64 + 4 * (l0 >> 4) + (t < 8 ? ((2 * half) ^ (t >> 1)) : 2 * half + (lane >> 5)) * 16) * 2 + (pc < 162 ? 0 : 1u << 30);
    __builtin_amdgcn_raw_buffer_store_b64(u32x2{cvtpk(a[0], a[1]), cvtpk(a[2], a[3])}, so, ov, 0, 0);
    __builtin_amdgcn_raw_buffer_store_b64(u32x2{cvtpk(a[4], a[5]), cvtpk(a[6], a[7])}, so, t < 8 ? ov ^ 32 : ov + 2 * DM * 2, 0, 0);
  }
}

struct Out2 { bf16x8 of[16][2], t8[4]; bf16x4 wh[32], nh[14]; f32x4 acc[4][2], acc8; u32x4 z[4]; u32x2 z8; unsigned tb[4], ca[4], ta, sl; int ns, nx; };
DI void p2_pack(Out2& o, int k, int e) { const f32x4 u = o.acc[2 * (k >> 1) + (e >> 1)][k & 1]; unsigned v = cvtpk(u[2 * (e & 1)], u[2 * (e & 1) + 1]); pin1(v); o.z[k][e] = v; }
DI void p2_put(Out2& o, int k, srd_t sp, unsigned pv) { __builtin_amdgcn_raw_buffer_store_b128(o.z[k], sp, pv + (k & 1) * 2048 + (k >> 1) * 1024, 0, 16); }
template <int R> DI void p2_sub(Out2& o, srd_t sp, unsigned& pv, unsigned& pv8) {
  if constexpr ((R & 1) == 0) bar();
  static_for<36>([&](auto ic) {
    constexpr int m = decltype(ic)::value, j = m < 32 ? m >> 3 : 3, tt = (m >> 2) & 1, k = m & 3;
    const bf16x8 w = cat(o.wh[8 * j + 2 * k], o.wh[8 * j + 2 * k + 1]);
    f32x4& acc = m < 32 ? o.acc[j][tt] : o.acc8; const bf16x8 ob = m < 32 ? o.of[4 * R + k][tt] : o.t8[k];
    if constexpr (R == 0 && k == 0) acc = mfma(w, ob, f32x4{0.f, 0.f, 0.f, 0.f}); else mfma_acc(acc, w, ob);
    SB();
    if constexpr (m == 7 || m == 15 || m == 23) lg_wait<5>();
    if constexpr (m == 35) lg_wait<5>();
    if constexpr (m == 0) { o.nx = o.ns + 4; pin_s1(o.nx); }
    if constexpr (m == 1) { o.ns = o.ns == 32 ? 0 : o.nx; pin_s1(o.ns); }
    if constexpr (m == 2) { o.sl = (unsigned)(SLOT_TAB >> o.ns); pin_s1(o.sl); }
    if constexpr (m == 3) { o.sl &= 15; pin_s1(o.sl); }
    if constexpr (m == 4) { o.sl <<= 14; pin_s1(o.sl); }
    SB();
    constexpr int mn = m < 18 ? m + 14 : m + 10;
    if constexpr (m >= 18 && m < 22) o.t8[m - 18] = lds_b128(o.ta + (4 * R + m - 18) * 1024);
    else (mn < 32 ? o.wh[mn] : o.nh[mn - 32]) = __builtin_amdgcn_ds_read_tr16_b64_v4bf16((LAS bf16x4*)(o.ca[(mn >> 3) & 3] + ((mn & 7) >> 1) * 4096 + (mn & 1) * 2048));
    SB();
    if constexpr (m == 5 || m == 6 || m == 10 || m == 18) { constexpr int a = m == 5 ? 0 : m == 6 ? 1 : m == 10 ? 2 : 3; o.ca[a] = o.tb[a] + o.sl; pin1(o.ca[a]); }
    if constexpr (R == 3 && m >= 11 && m < 35 && ((m - 11) & 7) < 4) p2_pack(o, 2 * ((m - 11) >> 4) + (((m - 11) >> 1) & 1), 2 * (((m - 11) >> 3) & 1) + ((m - 11) & 1));
    if constexpr (R == 0 && m >= 11 && m < 15) p2_pack(o, 2 + ((m - 11) >> 1), 2 + ((m - 11) & 1));
    if constexpr (R == 0 && (m == 16 || m == 17)) { unsigned v = cvtpk(o.acc8[2 * (m & 1)], o.acc8[2 * (m & 1) + 1]); pin1(v); o.z8[m & 1] = v; }
    if constexpr (R < 3 && m == 8) p2_put(o, R == 0 ? 0 : R + 1, sp, pv);
    if constexpr (R == 0 && m == 26) p2_put(o, 1, sp, pv);
    if constexpr (R == 1 && m == 26) __builtin_amdgcn_raw_buffer_store_b64(o.z8, sp, pv8, 0, 16);
    if constexpr (R == 3 && (m == 24 || m == 25)) { unsigned& x = m == 24 ? pv : pv8; x += 18 * 1024; pin1(x); }
    if constexpr (m == 35) static_for<14>([&](auto qc) { o.wh[decltype(qc)::value] = o.nh[decltype(qc)::value]; });
    SB();
  });
}
DI void p2_main(const K1Params& prm, Out2& o, int wave, int ln) {
  const srd_t sp = make_srd(prm.ws + WS_PART + (size_t)blockIdx.x * 162 * 1024, 162 * 1024);
  unsigned pv = wave * 4096 + ln * 16 - 18 * 1024, pv8 = (3 ^ wave) * 512 + ln * 8 - 2048;
  static_for<4>([&](auto ic) { constexpr int j = decltype(ic)::value; const int jj = j ^ wave;
    o.tb[j] = (p2_trbase(ln) ^ ((jj & 1) << 5)) + (jj >> 1) * 512; o.ca[j] = o.tb[j] + sub_slot(0); o.z[j] = u32x4{0, 0, 0, 0}; });
  o.ns = 0; o.ta = OT + ln * 16;
  o.acc[3][0] = o.acc[3][1] = o.acc8 = f32x4{0.f, 0.f, 0.f, 0.f};
  bar();
  pin2(pv, pv8);
  static_for<14>([&](auto ic) { constexpr int i = decltype(ic)::value; o.wh[i] = __builtin_amdgcn_ds_read_tr16_b64_v4bf16((LAS bf16x4*)(o.ca[i >> 3] + ((i & 7) >> 1) * 4096 + (i & 1) * 2048)); });
  lg_wait<6>();
#pragma unroll 1
  for (int c = 0; c < 9; ++c) { p2_sub<0>(o, sp, pv, pv8); p2_sub<1>(o, sp, pv, pv8); p2_sub<2>(o, sp, pv, pv8); p2_sub<3>(o, sp, pv, pv8); }
  asm volatile("s_nop 7\n\ts_nop 1" ::: "memory"); SB();
  p2_put(o, 0, sp, pv); p2_put(o, 1, sp, pv);
  static_for<4>([&](auto ic) { p2_pack(o, 2 + (decltype(ic)::value >> 1), 2 + (decltype(ic)::value & 1)); });
  p2_put(o, 2, sp, pv); p2_put(o, 3, sp, pv);
  __builtin_amdgcn_raw_buffer_store_b64(u32x2{cvtpk(o.acc8[0], o.acc8[1]), cvtpk(o.acc8[2], o.acc8[3])}, sp, pv8, 0, 16);
}
template <class F> DI void p2_help(const K1Params& prm, const int j, const int ln, F&& early) {
  const Item it = item_of();
  const char* wo = (const char*)(it.p ? prm.wo[1] : prm.wo[0]);
  const P2Src v = p2_src(ln);
  int q = 0, qs = 0;
  auto sub = [&]() { p2_issue4(sub_slot(qs) + j * 4096, v, make_srd(wo, q < 36 ? NH * DH * DM * 2 : 0), p2_so(it, 3 - (q & 3), q >> 2) + j * 32 * (DM * 2)); ++q; qs = qs == 8 ? 0 : qs + 1; };
  sub(); sub(); sub(); sub();
  early();
  vm_bar<4>(); sub(); sub();
#pragma unroll 1
  for (int i = 0; i < 18; ++i) { vm_bar<8>(); sub(); sub(); }
}

#define CODE_BYTES 145000
template <int NT> DI void attn_wave(const K1Params& prm, const int wave, const int lane, const void* code) {
  const int g = lane >> 4, r = lane & 15, tile0 = 2 * wave;
  Attn<NT> a;
  const unsigned tr0 = g * 1024 + (r >> 2) * 64 + (g & 1) * 32 + 8 * (r & 3);
  a.ka = (r >> 2) * 1024 + (r & 3) * 64 + ((g ^ ((r >> 1) & 2)) << 4); a.va0 = 8192 + tr0; a.va1 = a.va0 ^ 32; a.qa = XREG + tile0 * 4096 + lane * 16;
  a.w0 = a.w1 = OREG + wave * 4096 + lane * 16; a.wd = OREG - XREG - 6 * 4096;
  { const u32x4 o = {0x3F803F80u, 0x3F803F80u, 0x3F803F80u, 0x3F803F80u}; a.ones = __builtin_bit_cast(bf16x8, o); asm volatile("" : "+v"(a.ones)); }
#pragma unroll
  for (int t = 0; t < NT; ++t) {
    a.P[t] = u32x4{0, 0, 0, 0}; a.L[t] = f32x4{1.f, 1.f, 1.f, 1.f};
#pragma unroll
    for (int cc = 0; cc < 8; ++cc) a.O[cc][t] = f32x4{0.f, 0.f, 0.f, 0.f};
  }
  {
    unsigned sink = 0;
    touch(sink, (((blockIdx.x >> 3) % 18) * 64 + lane) * 128, make_srd(code, wave == 1 ? CODE_BYTES : 0));
    if (wave == 0) hs_request(prm.ws, lane);
#pragma unroll 1
    for (int i = 0; i < 18; ++i) {
      bar();
    }
    asm volatile("s_waitcnt vmcnt(0)" : "+v"(sink)::"memory");
  }
  f32x4 SA[2][NT], SC[2][NT];
  bar();
#pragma unroll
  for (int i = 0; i < 4 * NT; ++i) a.Q[i & 3][i >> 2] = lds_b128(a.qa + i * 1024);
  interval<NT, 15, 1>(a, SC, SA);
#pragma unroll 1
  for (int hh = 0; hh < 4; ++hh) {
    auto step = [&](auto ic) {
      constexpr int N = decltype(ic)::value;
      bar();
      if constexpr (N & 1) interval<NT, N, 0>(a, SC, SA); else interval<NT, N, 0>(a, SA, SC);
    };
    if constexpr (NT == 2) static_for<16>(step);
    else {
      step(IC<0>{}); step(IC<1>{});
#pragma unroll 1
      for (int r = 0; r < 3; ++r) { step(IC<2>{}); step(IC<3>{}); step(IC<4>{}); step(IC<5>{}); }
      step(IC<14>{}); step(IC<15>{});
    }
    if (hh == 2) { a.wd = 0; if constexpr (NT == 2) { a.w0 = a.qa; a.w1 = a.qa + 4096; } }
  }
  bar();
  interval<NT, 0, 2>(a, SA, SC);
  Out2 o;
  const int ln = lane_id();
  static_for<8>([&](auto ic) { constexpr int t = decltype(ic)::value >> 2, k = decltype(ic)::value & 3;
    o.of[4 + k][t] = a.Q[k][t]; o.of[8 + k][t] = lds_b128(park1(tile0 + t) + k * 1024 + ln * 16); o.of[12 + k][t] = lds_b128(park0(tile0 + t) + k * 1024 + ln * 16); });
  static_for<4 * NT>([&](auto ic) { constexpr int i = (decltype(ic)::value + 8) % (4 * NT);
    const f32x4 u = a.OF[2 * (i & 3)][i >> 2] * a.LF[i >> 2][0], w = a.OF[2 * (i & 3) + 1][i >> 2] * a.LF[i >> 2][0];
    const u32x4 z = {cvtpk(u[0], u[1]), cvtpk(u[2], u[3]), cvtpk(w[0], w[1]), cvtpk(w[2], w[3])};
    if constexpr (i < 8) o.of[i & 3][i >> 2] = __builtin_bit_cast(bf16x8, z); else *(LAS u32x4*)(OT + (i - 8) * 1024 + ln * 16) = z; });
  lg_wait<0>();
  p2_main(prm, o, wave, ln);
  reduce_out(prm, wave, lane_id());
}

#define TPACE 63
#define FCAP 22
#define PACE_0 18
#define PACE_1 66
DI void load_wave(const K1Params& prm, const int lane) {
  const Item it = item_of();
  const int seg = lane >> 4, row = (lane >> 2) & 3;
  Loader l; w_setup(l, prm, lane);
  l.col += 256; l.row += 3 * 32 * 8192; if (l.row >= 18 * 32 * 8192) l.row -= 18 * 32 * 8192;
  const size_t kvoff = (size_t)(it.b * NH + it.h0) * SK * DH * 2;
  l.sK = make_srd((const char*)(it.p ? prm.k[1] : prm.k[0]) + kvoff, 4 * SK * DH * 2); l.sV = make_srd((const char*)(it.p ? prm.v[1] : prm.v[0]) + kvoff, 4 * SK * DH * 2);
  l.v0 = row * 256 + seg * 64 + ((lane & 3) << 4); l.v1 = l.v0 ^ 32;
  l.rk = (blockIdx.x + (blockIdx.x >> 4)) & 15;
  unsigned la = OREG + lane * 16;
  unsigned tlast = (unsigned)__builtin_amdgcn_s_memrealtime(), tmin = (unsigned)__builtin_amdgcn_s_memtime();
  u32x4 H0[36], H1[24];
  vm_bar<16>();
  tmin = (unsigned)__builtin_amdgcn_s_memtime() - tmin < ((unsigned)__builtin_amdgcn_s_memrealtime() - tlast) * FCAP ? 0 : TPACE;
  issue_w<3>(l); issue_w<4>(l); issue_w<5>(l);
  static_for<82>([&](auto ic) {
    constexpr int pi = decltype(ic)::value, hh = (pi - 18) >> 4, N = (pi - 18) & 15;
    if constexpr (pi > PACE_0 && pi <= PACE_1) { while ((unsigned)__builtin_amdgcn_s_memrealtime() - tlast < tmin) __builtin_amdgcn_s_sleep(1); }
    vm_bar<l_count(pi)>();
    tlast = (unsigned)__builtin_amdgcn_s_memrealtime();
    issue_w_interval<pi>(l);
    if constexpr (pi >= 14) issue_unit<pi - 14>(l);
    if constexpr (pi >= 18) {
      asm volatile("" : "+v"(la));
      if constexpr ((hh == 1 || hh == 2) && N == 2) static_for<12>([&](auto jc) { constexpr int j = decltype(jc)::value; (hh == 1 ? H0[24 + j] : H1[12 + j]) = *(LAS u32x4*)(la + j * 1024); });
      if constexpr ((hh == 1 || hh == 2) && N >= 4 && N < 8) static_for<3>([&](auto jc) { constexpr int w = decltype(jc)::value; (hh == 1 ? H0[8 * w + N - 4] : H1[4 * w + N - 4]) = *(LAS u32x4*)(la + w * 4096 + (N - 4) * 1024); });
      if constexpr (hh == 1 && N >= 8 && N < 12) static_for<3>([&](auto jc) { constexpr int w = decltype(jc)::value; H0[8 * w + N - 4] = *(LAS u32x4*)(la + w * 4096 + (N - 8) * 1024); });
      if constexpr (hh == 3 && N == 0) la -= OREG - WREG;
      if constexpr (hh == 3 && N == 0) static_for<4>([&](auto jc) { constexpr int j = decltype(jc)::value; *(LAS u32x4*)(la + j * 1024) = H1[20 + j]; *(LAS u32x4*)(la + 4096 + j * 1024) = H0[32 + j]; });
      if constexpr (hh == 3 && N >= 3 && N < 16) static_for<4>([&](auto jc) {
        constexpr int j = decltype(jc)::value, e = N - 3;
        if constexpr (e < 8) *(LAS u32x4*)(la + e * 4096 + j * 1024) = H0[4 * e + j];
        else if constexpr (e < 12) *(LAS u32x4*)(la + e * 4096 + j * 1024) = H1[4 * (e - 8) + j];
        else *(LAS u32x4*)(la + (X8 - WREG) + j * 1024) = H1[16 + j]; });
      lg_wait<0>(); SB();
    }
  });
  vm_bar<0>();
  p2_help(prm, 3, lane_id(), []() {});
  reduce_out(prm, 7, lane_id());
}

struct Proj {
  f32x4 A[8][3];
  bf16x8 xr[NRES][2][3], xs[2][2][3], wf[8];
  u32x2 hold[2]; bf16x4 lo;
  unsigned wa0, wa1, xa, qw;
  srd_t sX; unsigned xl, vx[3], u, so;
};
template <int DS, int PC> DI void issue_xp(Proj& q) {
  constexpr int HH = PC / 3, I = PC % 3;
  if constexpr (I == 0)
    asm volatile("s_add_u32 m0, %1, %5\n\ts_add_u32 %0, %4, %6\n\tbuffer_load_dwordx4 %2, %3, %0 offen lds"
                 : "=&s"(q.so) : "s"(q.xl), "v"(q.vx[0]), "s"(q.sX), "s"(q.u), "i"((2 * DS + HH) * 3072), "i"(HH * 8 * DM * 2) : "memory", "scc");
  else
    asm volatile("buffer_load_dwordx4 %0, %1, %2 offen offset:%3 lds" ::"v"(q.vx[I]), "s"(q.sX), "s"(q.so), "i"(I * 1024) : "memory");
  if constexpr (PC == 5) q.u = q.u == 8 * 128 ? 0 : q.u + 128;
}
template <int DS> DI void issue_x(Proj& q) { issue_xp<DS, 0>(q); issue_xp<DS, 1>(q); issue_xp<DS, 2>(q); issue_xp<DS, 3>(q); issue_xp<DS, 4>(q); issue_xp<DS, 5>(q); }
DI void q_scale(Proj& q, int i) { f32x4 u = q.A[i / 3][i % 3]; float a = u[0] * QSCALE, b = u[1] * QSCALE, c = u[2] * QSCALE, d = u[3] * QSCALE;
  asm volatile("" : "+v"(a), "+v"(b), "+v"(c), "+v"(d)); q.A[i / 3][i % 3] = f32x4{a, b, c, d}; }
DI void q_pack(Proj& q, int i) { const f32x4 u = q.A[i / 3][i % 3]; u32x2 z = {cvtpk(u[0], u[1]), cvtpk(u[2], u[3])}; asm volatile("" : "+v"(z)); q.hold[i & 1] = z; }
DI void q_write(Proj& q, int i) {
  const int cc = i / 3, t = i % 3; *(LAS u32x2*)(q.qw + (t * 4 + (cc >> 1)) * 1024 + (cc & 1) * 512) = q.hold[i & 1]; }
template <int I> DI void q_gap(Proj& q) {
  if constexpr (I >= 2 && I < 26) { q_write(q, I - 2); SB(); }
  if constexpr (I >= 1 && I < 25) q_pack(q, I - 1);
  if constexpr (I >= 0 && I < 24) q_scale(q, I);
}
constexpr int CVD = 3;
template <int U, bool FIRST> DI bf16x8& xreg(Proj& q, int par, int t) { if constexpr (U < NRES) return q.xr[U][par][t]; else return q.xs[U & 1][par][t]; }
template <bool FIRST> DI void proj_head(Proj& q) {
  constexpr int U0 = FIRST ? 0 : NRES;
  static_for<16>([&](auto ip) {
    constexpr int PI = decltype(ip)::value;
    if constexpr (PI == 1) __builtin_amdgcn_s_waitcnt(0xC07F);
    bar();
    pin4(q.wa0, q.wa1, q.xa, q.qw);
    if constexpr (!FIRST && PI == 2) { q.u += NRES * 128; if (q.u >= 9 * 128) q.u -= 9 * 128; }
    constexpr int G0 = gstart(PI), NG = (PI == 15 ? 144 : gstart(PI + 1)) - G0;
    static_for<NG>([&](auto ig) {
      constexpr int G = G0 + decltype(ig)::value, S = G / 8, cc = G % 8, U = S / 2, GN = (G + RW) % 144;
      static_for<3>([&](auto it) {
        constexpr int t = decltype(it)::value;
        if constexpr (S == 0) q.A[cc][t] = mfma(q.wf[G % 8], xreg<U, FIRST>(q, 0, t), f32x4{0.f, 0.f, 0.f, 0.f});
        else mfma_acc(q.A[cc][t], q.wf[G % 8], xreg<U, FIRST>(q, S & 1, t));
        SB();
        if constexpr (t < 2) {
          const bf16x4 hv = __builtin_amdgcn_ds_read_tr16_b64_v4bf16((LAS bf16x4*)(((GN & 1) ? q.wa1 : q.wa0) + ((GN / 8) % 6) * 8192 + ((GN % 8) >> 1) * 256 + t * 4096));
          if constexpr (t == 0) q.lo = hv; else q.wf[(G + RW) % 8] = __builtin_shufflevector(q.lo, hv, 0, 1, 2, 3, 4, 5, 6, 7);
          SB();
        }
        if constexpr (G >= 136 + CVD) q_gap<3 * (G - 136 - CVD) + t>(q);
        if constexpr (!FIRST && G <= CVD) q_gap<3 * (G + 8 - CVD) + t>(q);
        if constexpr ((S & 1) && U + 1 >= U0 && U + 1 < 9 && t == 2 && cc < 6) {
          if constexpr (cc == 0) { if constexpr (U + 2 < 9) asm volatile("s_waitcnt vmcnt(6)" ::: "memory"); else asm volatile("s_waitcnt vmcnt(0)" ::: "memory"); }
          xreg<U + 1, FIRST>(q, cc / 3, cc % 3) = lds_b128(q.xa + ((U + 1) & 1) * 6144 + (cc % 3) * 1024 + (cc / 3) * 512);
        }
        if constexpr (!(S & 1) && U >= U0 && U + 2 < 9 && cc >= 1 && cc < 7 && t == 2) {
          if constexpr (cc == 1) lg_wait<8>();
          issue_xp<U & 1, cc - 1>(q);
        }
        if constexpr (!FIRST && t == 2 && G >= gstart(2) && G < gstart(2) + 12) issue_xp<(U0 + (G - gstart(2)) / 6) & 1, (G - gstart(2)) % 6>(q);
        SB();
      });
    });
  });
}
DI void x_setup(Proj& q, const K1Params& prm, int wave, int lane) {
  const Item it = item_of();
  const int pw = wave - 4, vr = (lane >> 2) & 7;
  q.sX = make_srd((const char*)(it.p ? prm.x[1] : prm.x[0]) + (size_t)it.b * SQ * DM * 2, SQ * DM * 2);
  q.xl = XREG + pw * 12288; q.u = it.rot * 128;
#pragma unroll
  for (int i = 0; i < 3; ++i) q.vx[i] = (48 * pw + 16 * i + vr) * (DM * 2) + (lane >> 5) * 64 + (((lane & 3) ^ ((vr >> 2) << 1)) << 4) - 1024 * i;
}
DI void proj_wave(const K1Params& prm, const int wave, const int lane) {
  const int g = lane >> 4, r = lane & 15;
  Proj q;
  x_setup(q, prm, wave, lane);
  q.u += 256; if (q.u >= 9 * 128) q.u -= 9 * 128;
  q.wa0 = WREG + g * 1024 + (r >> 2) * 64 + (g & 1) * 32 + 8 * (r & 3); q.wa1 = q.wa0 ^ 32;
  q.xa = q.xl + (r >> 3) * 3072 + (r & 7) * 64 + ((g ^ ((r >> 1) & 2)) << 4); q.qw = q.xl + (g >> 1) * 256 + r * 16 + (g & 1) * 8;
  vm_bar<6>();
#pragma unroll
  for (int t = 0; t < 3; ++t) { q.xr[0][0][t] = lds_b128(q.xa + t * 1024); q.xr[0][1][t] = lds_b128(q.xa + t * 1024 + 512); }
#pragma unroll
  for (int i = 0; i < RW; ++i) q.wf[i] = rd_tr(q.wa0, q.wa1, 0, i);
  SB(); lg_wait<0>();
  proj_head<true>(q);
#pragma unroll 1
  for (int m = 1; m < 4; ++m) {
    proj_head<false>(q);
  }
  bar();
  asm volatile("s_nop 7" ::: "memory"); SB();
  static_for<3 * CVD + 2>([&](auto ic) { q_gap<3 * (8 - CVD) + decltype(ic)::value>(q); });
  lg_wait<0>();
  bar();
}
DI void help_wave(const K1Params& prm, const int wave, const int lane) {
  const Item it = item_of();
  const int pw = wave - 4, idx = blockIdx.x >> 3, hb = (blockIdx.x >> 1) & 1;
  const srd_t sw = make_srd(it.p ? prm.wo[1] : prm.wo[0], NH * DH * DM * 2);
  bf16x8 of[4];
  unsigned sink = 0;
  static_for<17>([&](auto ic) {
    constexpr int n = decltype(ic)::value;
    bar();
#define PFW0 14
    if constexpr (n >= PFW0 && n < PFW0 + 3) touch(sink, (hb * 2048 + idx * 64 + lane) * (DM * 2) + (3 * (n - PFW0) + pw) * 128, sw);
    if constexpr (n == 2) {
      static_for<4>([&](auto jc) { constexpr int j = decltype(jc)::value; of[j] = lds_b128((pw == 0 ? X8 : WREG + (pw - 1) * 4096) + j * 1024 + lane * 16); });
      lg_wait<0>();
    }
  });
  p2_help(prm, pw, lane, [&]() { static_for<4>([&](auto jc) { *(LAS bf16x8*)(OT + (pw + 1) * 4096 + decltype(jc)::value * 1024 + lane * 16) = of[decltype(jc)::value]; }); lg_wait<0>(); });
  keep(sink);
  reduce_out(prm, wave, lane_id());
}

__global__ __launch_bounds__(512, 2) void attn_kernel(K1Params prm) {
  const void* code; asm volatile("s_getpc_b64 %0" : "=s"(code));
  int wave = rfl(threadIdx.x >> 6);
  if (wave == 7) { Loader l; w_setup(l, prm, lane_id()); issue_w<0>(l); issue_w<1>(l); issue_w<2>(l); }
  else if (wave >= 4) { Proj q; x_setup(q, prm, wave, lane_id()); issue_x<0>(q); issue_x<1>(q); }
  asm volatile("s_nop 0" ::: "memory"); pin_s1(wave);
  if (wave < 3) attn_wave<2>(prm, wave, lane_id(), code);
  else if (wave == 3) attn_wave<3>(prm, wave, lane_id(), code);
  else if (wave == 7) {
    load_wave(prm, lane_id());
  }
  else {
    proj_wave(prm, wave, lane_id());
    help_wave(prm, wave, lane_id());
  }
}

extern "C" void kernel_launch(void* const* d_in, const int* in_sizes, int n_in, void* d_out, int out_size, void* d_ws, size_t ws_size,
                              hipStream_t stream) {
  (void)in_sizes; (void)n_in; (void)out_size; (void)ws_size;
  (void)hipFuncSetAttribute((const void*)attn_kernel, hipFuncAttributeMaxDynamicSharedMemorySize, (int)K1_LDS);
  K1Params prm;
  for (int p = 0; p < 2; ++p) {
    prm.x[p] = d_in[5 * p + 0]; prm.k[p] = d_in[5 * p + 1]; prm.v[p] = d_in[5 * p + 2];
    prm.wq[p] = d_in[5 * p + 3]; prm.wo[p] = d_in[5 * p + 4];
  }
  prm.ws = (char*)d_ws; prm.out = (unsigned short*)d_out;
  hipLaunchKernelGGL(attn_kernel, dim3(256), dim3(512), K1_LDS, stream, prm);
}
```
